```python
import torch

def _softmax(x, axis=-1):
    return torch.softmax(x, dim=axis)
def _gelu(x):
    return torch.nn.functional.gelu(x, approximate="tanh")
def _silu(x):
    return torch.nn.functional.silu(x)
def _sigmoid(x):
    return torch.sigmoid(x)
def _layernorm(x, axis=-1, eps=1e-5):
    m = x.mean(dim=axis, keepdim=True)
    v = x.var(dim=axis, keepdim=True, unbiased=False)
    return (x - m) / torch.sqrt(v + eps)
def _scatter_add(tgt, idx, src, axis=0):
    return tgt.clone().index_add_(axis, idx, src)
def _segment_sum(src, seg, n):
    out = torch.zeros((n,) + src.shape[1:], dtype=src.dtype)
    return out.index_add_(0, seg, src)
def _pad(x, pw):
    flat = tuple(v for pair in reversed(pw) for v in pair)
    return torch.nn.functional.pad(x, flat)

kernel_name = 'standalone__grn__sampling'

def setup_inputs():

    _w = (lambda a: (lambda t: t.to(torch.bfloat16) if t.is_floating_point() else t)(torch.as_tensor(a)))
    B, S, D = 1024, 8, 1024
    return {
        "x": _w((lambda *s: torch.randn(*s, dtype=torch.bfloat16) * 0.05)(B, S, D)),
        "g": _w((lambda *s: torch.randn(*s, dtype=torch.bfloat16) * 0.4)(D) * 0.1),
        "b": _w((lambda *s: torch.randn(*s, dtype=torch.bfloat16) * 0.1)(D) * 0.1),
    }

def reference(x, g, b):
    gx = torch.sqrt((x * x).sum(axis=-2, keepdims=True))
    nx = gx / (gx.mean(axis=-1, keepdims=True) + 1e-6)
    return g * (x * nx) + b + x
```

```cpp
#include <hip/hip_runtime.h>
#include <stdint.h>

typedef uint32_t u32x4 __attribute__((ext_vector_type(4)));
typedef float f32x2 __attribute__((ext_vector_type(2)));
typedef __bf16 bf16x2 __attribute__((ext_vector_type(2)));

constexpr int kRows = 8, kCols = 1024;
constexpr int kRowBytes = kCols * 2;
constexpr int kBatchBytes = kRows * kRowBytes;
constexpr int kPairs = 4;
constexpr int kWaves = 2 * kPairs;
constexpr int kMarkRow = 0;
constexpr int kSlot = 65;
constexpr uint32_t kEmpty = 0xbf800000u;
constexpr int kRsrcWord3 = 0x00020000;
constexpr int kLoadStream = 2 | 16;
constexpr int kStoreStream = 1 | 2 | 16;

__device__ __forceinline__ uint32_t f2u(float v) { return __builtin_bit_cast(uint32_t, v); }
__device__ __forceinline__ float u2f(uint32_t v) { return __builtin_bit_cast(float, v); }
__device__ __forceinline__ f32x2 unpack(uint32_t v) {
  return (f32x2){u2f(v << 16), u2f(v & 0xffff0000u)};
}
__device__ __forceinline__ uint32_t pack(f32x2 v) {
  return __builtin_bit_cast(uint32_t, __builtin_convertvector(v, bf16x2));
}
template <int CTRL>
__device__ __forceinline__ float dpp_add(float v) {
  return v + u2f(__builtin_amdgcn_update_dpp(0, f2u(v), CTRL, 0xf, 0xf, true));
}
__device__ __forceinline__ void lds_put(uint32_t* p, uint32_t v) {
  __hip_atomic_store(p, v, __ATOMIC_RELAXED, __HIP_MEMORY_SCOPE_WORKGROUP);
}
__device__ __forceinline__ uint32_t lds_get(uint32_t* p) {
  return __hip_atomic_load(p, __ATOMIC_RELAXED, __HIP_MEMORY_SCOPE_WORKGROUP);
}

__global__ __attribute__((aligned(4096))) __launch_bounds__(64 * kWaves)
void grn_kernel(const uint16_t* __restrict__ x, const uint16_t* __restrict__ g,
                const uint16_t* __restrict__ b, uint16_t* __restrict__ out,
                uint32_t nblk, uint32_t total_bytes) {
  __builtin_amdgcn_s_setprio(3);

  __shared__ uint32_t flags[2 * kWaves * kSlot];
  const int tid = threadIdx.x;
  const int wave = __builtin_amdgcn_readfirstlane(tid >> 6);
  uint32_t* const slot = flags + tid + wave;
  lds_put(slot, kEmpty);
  lds_put(slot + kWaves * kSlot, kEmpty);
  uint32_t col = (tid & 127) * 16;
  asm volatile("" : "+v"(col));
  __syncthreads();
  __builtin_amdgcn_sched_barrier(0);

  const uint32_t off = (blockIdx.x + (wave >> 1) * nblk) * kBatchBytes + col;
  const __amdgpu_buffer_rsrc_t rx = __builtin_amdgcn_make_buffer_rsrc((void*)x, 0, total_bytes, kRsrcWord3);
  const __amdgpu_buffer_rsrc_t rg = __builtin_amdgcn_make_buffer_rsrc((void*)g, 0, kRowBytes, kRsrcWord3);
  const __amdgpu_buffer_rsrc_t rb = __builtin_amdgcn_make_buffer_rsrc((void*)b, 0, kRowBytes, kRsrcWord3);
  const __amdgpu_buffer_rsrc_t ro = __builtin_amdgcn_make_buffer_rsrc((void*)out, 0, total_bytes, kRsrcWord3);

  u32x4 xv[kRows];
#pragma unroll
  for (int j = 0; j < kRows; ++j)
    xv[j] = __builtin_amdgcn_raw_buffer_load_b128(rx, off + (j & 1) * kRowBytes,
                                                  (j >> 1) * (2 * kRowBytes), kLoadStream);
  asm volatile("" ::: "memory");
  const u32x4 gv = __builtin_amdgcn_raw_buffer_load_b128(rg, col, 0, 0);
  const u32x4 bv = __builtin_amdgcn_raw_buffer_load_b128(rb, col, 0, 0);
  asm volatile("" ::: "memory");
  __builtin_amdgcn_sched_barrier(0);
  __builtin_amdgcn_s_setprio(0);
  __builtin_amdgcn_sched_barrier(0);

  f32x2 xf[kRows][4], ss[4];
#pragma unroll
  for (int j = 0; j < kRows; ++j) {
#pragma unroll
    for (int k = 0; k < 4; ++k) {
      xf[j][k] = unpack(xv[j][k]);
      ss[k] = j == 0 ? xf[j][k] * xf[j][k] : __builtin_elementwise_fma(xf[j][k], xf[j][k], ss[k]);
    }
    asm volatile("" : "+v"(ss[0]), "+v"(ss[1]), "+v"(ss[2]), "+v"(ss[3]));
    if (j == kMarkRow) lds_put(slot + kWaves * kSlot, 0u);
    __builtin_amdgcn_sched_barrier(0);
  }

  f32x2 gx[4];
#pragma unroll
  for (int k = 0; k < 4; ++k)
    gx[k] = (f32x2){__builtin_amdgcn_sqrtf(ss[k].x), __builtin_amdgcn_sqrtf(ss[k].y)};
  const f32x2 p2 = (gx[0] + gx[1]) + (gx[2] + gx[3]);
  float part = p2.x + p2.y;
  part = dpp_add<0x111>(part);
  part = dpp_add<0x112>(part);
  part = dpp_add<0x114>(part);
  part = dpp_add<0x118>(part);
  part = dpp_add<0x142>(part);
  part = dpp_add<0x143>(part);
  lds_put(slot, f2u(part));
  __builtin_amdgcn_sched_barrier(0);

  f32x2 gg[4], bf[4];
#pragma unroll
  for (int k = 0; k < 4; ++k) {
    gg[k] = unpack(gv[k]) * gx[k];
    bf[k] = unpack(bv[k]);
  }
  asm volatile("" : "+v"(gg[0]), "+v"(gg[1]), "+v"(gg[2]), "+v"(gg[3]),
                    "+v"(bf[0]), "+v"(bf[1]), "+v"(bf[2]), "+v"(bf[3]));

  uint32_t* const poll = flags + (tid & (2 * kWaves - 1)) * kSlot + 63;
  const uint32_t need = (3u << (wave & ~1)) | (((1u << kWaves) - 1) << kWaves);
  uint32_t word = lds_get(poll);
  while (((uint32_t)__ballot(word != kEmpty) & need) != need) {
    __builtin_amdgcn_s_sleep(1);
    word = lds_get(poll);
  }
  const float pair_total = dpp_add<0xb1>(u2f(word));
  const float inv_lane = __builtin_amdgcn_rcpf(pair_total * (1.0f / kCols) + 1e-6f);
  const float inv = u2f(__builtin_amdgcn_readlane(f2u(inv_lane), wave));

  f32x2 cf[4];
#pragma unroll
  for (int k = 0; k < 4; ++k)
    cf[k] = __builtin_elementwise_fma(gg[k], (f32x2){inv, inv}, (f32x2){1.f, 1.f});
#pragma unroll
  for (int j = 0; j < kRows; ++j) {
    u32x4 o;
#pragma unroll
    for (int k = 0; k < 4; ++k) o[k] = pack(__builtin_elementwise_fma(xf[j][k], cf[k], bf[k]));
    __builtin_amdgcn_raw_buffer_store_b128(o, ro, off + j * kRowBytes, 0, kStoreStream);
  }
}

extern "C" void kernel_launch(void* const* d_in, const int* in_sizes, int n_in,
                              void* d_out, int out_size, void* d_ws, size_t ws_size,
                              hipStream_t stream) {
  const uint32_t nbatch = in_sizes[0] / (kRows * kCols);
  const uint32_t nblk = (nbatch + kPairs - 1) / kPairs;
  grn_kernel<<<nblk, 64 * kWaves, 0, stream>>>((const uint16_t*)d_in[0], (const uint16_t*)d_in[1],
                                               (const uint16_t*)d_in[2], (uint16_t*)d_out,
                                               nblk, nbatch * kBatchBytes);
}
```
